```python
import torch

def _softmax(x, axis=-1):
    return torch.softmax(x, dim=axis)
def _gelu(x):
    return torch.nn.functional.gelu(x, approximate="tanh")
def _silu(x):
    return torch.nn.functional.silu(x)
def _sigmoid(x):
    return torch.sigmoid(x)
def _layernorm(x, axis=-1, eps=1e-5):
    m = x.mean(dim=axis, keepdim=True)
    v = x.var(dim=axis, keepdim=True, unbiased=False)
    return (x - m) / torch.sqrt(v + eps)
def _scatter_add(tgt, idx, src, axis=0):
    return tgt.clone().index_add_(axis, idx, src)
def _segment_sum(src, seg, n):
    out = torch.zeros((n,) + src.shape[1:], dtype=src.dtype)
    return out.index_add_(0, seg, src)
def _pad(x, pw):
    flat = tuple(v for pair in reversed(pw) for v in pair)
    return torch.nn.functional.pad(x, flat)

kernel_name = 'standalone__gqa_block_cached__decode'

def setup_inputs():

    _w = (lambda a: (lambda t: t.to(torch.bfloat16) if t.is_floating_point() else t)(torch.as_tensor(a)))
    B, Sq, D = 1, 1, 512
    Hq, Hkv, Sk, Dh = 32, 8, 65536, 128
    assert Hq % Hkv == 0, (Hq, Hkv)
    return {
        "x": _w((lambda *s: torch.randn(*s, dtype=torch.bfloat16) * 0.8)(B, Sq, D)),
        "k_cache": _w((lambda *s: torch.randn(*s, dtype=torch.bfloat16) * 0.8)(B, Hkv, Sk, Dh)),
        "v_cache": _w((lambda *s: torch.randn(*s, dtype=torch.bfloat16) * 0.05)(B, Hkv, Sk, Dh)),
        "wq": _w((lambda *s: torch.randn(*s, dtype=torch.bfloat16) * 0.8)(D, Hq * Dh) * (D ** -0.5)),
        "wo": _w((lambda *s: torch.randn(*s, dtype=torch.bfloat16) * 0.05)(Hq * Dh, D) * ((Hq * Dh) ** -0.5)),
    }

def reference(x, k_cache, v_cache, wq, wo):
    B, Sq, D = x.shape
    Hq, Dh = 32, 128
    Hkv = k_cache.shape[1]
    rep = Hq // Hkv
    q = (lambda x, *d: x.permute(*d))((x @ wq).reshape(B, Sq, Hq, Dh), 0, 2, 1, 3)
    k = (lambda x, n, axis: torch.repeat_interleave(x, n, dim=axis))(k_cache, rep, axis=1)
    v = (lambda x, n, axis: torch.repeat_interleave(x, n, dim=axis))(v_cache, rep, axis=1)
    scores = torch.einsum("bhqd,bhkd->bhqk", q, k) * (Dh ** -0.5)
    p = _softmax(scores, axis=-1)
    o = torch.einsum("bhqk,bhkd->bhqd", p, v)
    return (lambda x, *d: x.permute(*d))(o, 0, 2, 1, 3).reshape(B, Sq, Hq * Dh) @ wo
```

```cpp
#include <hip/hip_runtime.h>
#include <hip/hip_bf16.h>
#include <stdint.h>

typedef __attribute__((ext_vector_type(8))) __bf16 bf16x8;
typedef __attribute__((ext_vector_type(4))) float f32x4;
typedef __attribute__((ext_vector_type(4))) unsigned u32x4;

constexpr int D = 512, HQ = 32, HKV = 8, SK = 65536, DH = 128, G = 4;
constexpr int NSPLIT = 32;
constexpr int NW = 8;
constexpr int KEYS_PER_WG = SK / NSPLIT;
constexpr int NB = KEYS_PER_WG / (32 * NW);

__device__ __forceinline__ float bf2f(unsigned short v) { return __builtin_bit_cast(float, (unsigned)v << 16); }
__device__ __forceinline__ unsigned cvtpk(float lo, float hi) {
  typedef __attribute__((ext_vector_type(2))) __bf16 bf16x2;
  bf16x2 r = {(__bf16)lo, (__bf16)hi};
  return __builtin_bit_cast(unsigned, r);
}
__device__ __forceinline__ float xg_max(float x) {
  auto s = __builtin_amdgcn_permlane16_swap(__float_as_uint(x), __float_as_uint(x), false, false);
  x = fmaxf(__uint_as_float(s[0]), __uint_as_float(s[1]));
  auto t = __builtin_amdgcn_permlane32_swap(__float_as_uint(x), __float_as_uint(x), false, false);
  return fmaxf(__uint_as_float(t[0]), __uint_as_float(t[1]));
}
__device__ __forceinline__ float xg_sum(float x) {
  auto s = __builtin_amdgcn_permlane16_swap(__float_as_uint(x), __float_as_uint(x), false, false);
  x = __uint_as_float(s[0]) + __uint_as_float(s[1]);
  auto t = __builtin_amdgcn_permlane32_swap(__float_as_uint(x), __float_as_uint(x), false, false);
  return __uint_as_float(t[0]) + __uint_as_float(t[1]);
}

__global__ void __launch_bounds__(256) k_qproj(const unsigned short* __restrict__ x, const unsigned short* __restrict__ wq,
                                               unsigned short* __restrict__ q) {
  __shared__ float red[4][16];
  const int tid = threadIdx.x, lane = tid & 63, wave = tid >> 6;
  const int col0 = blockIdx.x * 16 + (tid & 1) * 8, rg = tid >> 1;
  float acc[8];
#pragma unroll
  for (int k = 0; k < 8; ++k) acc[k] = 0.f;
#pragma unroll
  for (int i = 0; i < 4; ++i) {
    const int row = rg + 128 * i;
    const u32x4 w = *(const u32x4*)(wq + (size_t)row * (HQ * DH) + col0);
    const float xv = bf2f(x[row]);
#pragma unroll
    for (int k = 0; k < 4; ++k) {
      acc[2 * k] = fmaf(xv, __builtin_bit_cast(float, w[k] << 16), acc[2 * k]);
      acc[2 * k + 1] = fmaf(xv, __builtin_bit_cast(float, w[k] & 0xffff0000u), acc[2 * k + 1]);
    }
  }
#pragma unroll
  for (int k = 0; k < 8; ++k) {
#pragma unroll
    for (int o = 2; o < 64; o <<= 1) acc[k] += __shfl_xor(acc[k], o);
  }
  if (lane < 2) {
#pragma unroll
    for (int k = 0; k < 8; ++k) red[wave][lane * 8 + k] = acc[k];
  }
  __syncthreads();
  if (tid < 16) {
    const float s = red[0][tid] + red[1][tid] + red[2][tid] + red[3][tid];
    q[blockIdx.x * 16 + tid] = (unsigned short)(cvtpk(s, 0.f) & 0xffff);
  }
}

__global__ void __launch_bounds__(NW * 64) k_attn(const unsigned short* __restrict__ qg, const unsigned short* __restrict__ kc,
                                                   const unsigned short* __restrict__ vc, float* __restrict__ part) {
  __shared__ float lds[NW * G * (DH + 4)];
  const int tid = threadIdx.x, lane = tid & 63, wave = __builtin_amdgcn_readfirstlane(tid >> 6);
  const int h = blockIdx.x & 7, sp = blockIdx.x >> 3;
  const int r = lane & 15, g = lane >> 4;
  constexpr float C2 = 0.08838834764831845f * 1.4426950408889634f;
  u32x4 qf[4];
#pragma unroll
  for (int s = 0; s < 4; ++s) {
    u32x4 t = {0u, 0u, 0u, 0u};
    if (r < G) t = *(const u32x4*)(qg + (h * G + r) * DH + (4 * s + g) * 8);
    qf[s] = t;
  }
  const size_t base = ((size_t)h * SK + (size_t)sp * KEYS_PER_WG) * DH;
  const unsigned short* kb = kc + base + (size_t)(4 * (r & 3) + (r >> 2)) * DH + g * 8;
  const unsigned short* vb = vc + base + lane * 8;
  u32x4 kf[8], vf[8];
  float m = -INFINITY, l = 0.f;
  f32x4 acc[8];
#pragma unroll
  for (int d = 0; d < 8; ++d) acc[d] = f32x4{0.f, 0.f, 0.f, 0.f};
  auto loadK = [&](int b) {
    const size_t off = (size_t)(b * NW + wave) * 32 * DH;
#pragma unroll
    for (int t = 0; t < 2; ++t)
#pragma unroll
      for (int s = 0; s < 4; ++s) kf[t * 4 + s] = __builtin_nontemporal_load((const u32x4*)(kb + off + t * 16 * DH + s * 32));
  };
  auto loadV = [&](int b) {
    const size_t off = (size_t)(b * NW + wave) * 32 * DH;
#pragma unroll
    for (int j = 0; j < 8; ++j) vf[j] = __builtin_nontemporal_load((const u32x4*)(vb + off + j * 4 * DH));
  };
  loadK(0);
  loadV(0);
#pragma unroll 1
  for (int b = 0; b < NB; ++b) {
    f32x4 s0 = {0.f, 0.f, 0.f, 0.f}, s1 = {0.f, 0.f, 0.f, 0.f};
#pragma unroll
    for (int s = 0; s < 4; ++s) {
      s0 = __builtin_amdgcn_mfma_f32_16x16x32_bf16(__builtin_bit_cast(bf16x8, kf[s]), __builtin_bit_cast(bf16x8, qf[s]), s0, 0, 0, 0);
      s1 = __builtin_amdgcn_mfma_f32_16x16x32_bf16(__builtin_bit_cast(bf16x8, kf[4 + s]), __builtin_bit_cast(bf16x8, qf[s]), s1, 0, 0, 0);
    }
    if (b + 1 < NB) loadK(b + 1);
    float mx = fmaxf(fmaxf(fmaxf(s0[0], s0[1]), fmaxf(s0[2], s0[3])), fmaxf(fmaxf(s1[0], s1[1]), fmaxf(s1[2], s1[3])));
    mx = xg_max(mx);
    if (__any(mx > m)) {
      const float mn = fmaxf(m, mx);
      const float alpha = __builtin_amdgcn_exp2f((m - mn) * C2);
      l *= alpha;
#pragma unroll
      for (int d = 0; d < 8; ++d) acc[d] *= alpha;
      m = mn;
    }
    const float mc = m * C2;
    float p[8];
#pragma unroll
    for (int i = 0; i < 4; ++i) {
      p[i] = __builtin_amdgcn_exp2f(fmaf(s0[i], C2, -mc));
      p[4 + i] = __builtin_amdgcn_exp2f(fmaf(s1[i], C2, -mc));
    }
    l += ((p[0] + p[1]) + (p[2] + p[3])) + ((p[4] + p[5]) + (p[6] + p[7]));
    const u32x4 pf = {cvtpk(p[0], p[1]), cvtpk(p[2], p[3]), cvtpk(p[4], p[5]), cvtpk(p[6], p[7])};
#pragma unroll
    for (int d = 0; d < 8; ++d) {
      const unsigned sel = (d & 1) ? 0x07060302u : 0x05040100u;
      u32x4 a;
#pragma unroll
      for (int u = 0; u < 4; ++u) a[u] = __builtin_amdgcn_perm(vf[2 * u + 1][d >> 1], vf[2 * u][d >> 1], sel);
      acc[d] = __builtin_amdgcn_mfma_f32_16x16x32_bf16(__builtin_bit_cast(bf16x8, a), __builtin_bit_cast(bf16x8, pf), acc[d], 0, 0, 0);
    }
    if (b + 1 < NB) loadV(b + 1);
  }
  l = xg_sum(l);
  if (r < G) {
    float* rec = lds + (wave * G + r) * (DH + 4);
#pragma unroll
    for (int i = 0; i < 4; ++i) {
      *(f32x4*)(rec + 4 + 8 * (4 * g + i)) = f32x4{acc[0][i], acc[1][i], acc[2][i], acc[3][i]};
      *(f32x4*)(rec + 4 + 8 * (4 * g + i) + 4) = f32x4{acc[4][i], acc[5][i], acc[6][i], acc[7][i]};
    }
    if (g == 0) { rec[0] = m; rec[1] = l; }
  }
  __syncthreads();
  {
    const int hh = tid >> 7, dd = tid & 127;
    float M = -INFINITY;
#pragma unroll
    for (int w = 0; w < NW; ++w) M = fmaxf(M, lds[(w * G + hh) * (DH + 4)]);
    float L = 0.f, o = 0.f;
#pragma unroll
    for (int w = 0; w < NW; ++w) {
      const float* rec = lds + (w * G + hh) * (DH + 4);
      const float wt = __builtin_amdgcn_exp2f((rec[0] - M) * C2);
      L = fmaf(wt, rec[1], L);
      o = fmaf(wt, rec[4 + dd], o);
    }
    float* dst = part + ((size_t)(h * G + hh) * NSPLIT + sp) * (DH + 4);
    dst[4 + dd] = o;
    if (dd == 0) { dst[0] = M; dst[1] = L; }
  }
}

__global__ void __launch_bounds__(128) k_combine(const float* __restrict__ part, unsigned short* __restrict__ o) {
  constexpr float C2 = 0.08838834764831845f * 1.4426950408889634f;
  const int hq = blockIdx.x, dd = threadIdx.x;
  const float* p = part + (size_t)hq * NSPLIT * (DH + 4);
  float M = -INFINITY;
  for (int s = 0; s < NSPLIT; ++s) M = fmaxf(M, p[s * (DH + 4)]);
  float L = 0.f, acc = 0.f;
  for (int s = 0; s < NSPLIT; ++s) {
    const float wt = __builtin_amdgcn_exp2f((p[s * (DH + 4)] - M) * C2);
    L = fmaf(wt, p[s * (DH + 4) + 1], L);
    acc = fmaf(wt, p[s * (DH + 4) + 4 + dd], acc);
  }
  o[hq * DH + dd] = (unsigned short)(cvtpk(acc / L, 0.f) & 0xffff);
}

__global__ void __launch_bounds__(256) k_oproj(const unsigned short* __restrict__ o, const unsigned short* __restrict__ wo,
                                               unsigned short* __restrict__ out) {
  __shared__ float red[4][8];
  const int tid = threadIdx.x, lane = tid & 63, wave = tid >> 6;
  const int col0 = blockIdx.x * 8;
  float acc[8];
#pragma unroll
  for (int k = 0; k < 8; ++k) acc[k] = 0.f;
#pragma unroll
  for (int i = 0; i < 16; ++i) {
    const int row = tid + 256 * i;
    const u32x4 w = *(const u32x4*)(wo + (size_t)row * D + col0);
    const float xv = bf2f(o[row]);
#pragma unroll
    for (int k = 0; k < 4; ++k) {
      acc[2 * k] = fmaf(xv, __builtin_bit_cast(float, w[k] << 16), acc[2 * k]);
      acc[2 * k + 1] = fmaf(xv, __builtin_bit_cast(float, w[k] & 0xffff0000u), acc[2 * k + 1]);
    }
  }
#pragma unroll
  for (int k = 0; k < 8; ++k) {
#pragma unroll
    for (int off = 1; off < 64; off <<= 1) acc[k] += __shfl_xor(acc[k], off);
  }
  if (lane == 0) {
#pragma unroll
    for (int k = 0; k < 8; ++k) red[wave][k] = acc[k];
  }
  __syncthreads();
  if (tid < 8) {
    const float s = red[0][tid] + red[1][tid] + red[2][tid] + red[3][tid];
    out[col0 + tid] = (unsigned short)(cvtpk(s, 0.f) & 0xffff);
  }
}

extern "C" void kernel_launch(void* const* d_in, const int* in_sizes, int n_in, void* d_out, int out_size, void* d_ws,
                              size_t ws_size, hipStream_t stream) {
  const unsigned short* x = (const unsigned short*)d_in[0];
  const unsigned short* kc = (const unsigned short*)d_in[1];
  const unsigned short* vc = (const unsigned short*)d_in[2];
  const unsigned short* wq = (const unsigned short*)d_in[3];
  const unsigned short* wo = (const unsigned short*)d_in[4];
  unsigned short* q = (unsigned short*)d_ws;
  unsigned short* o = (unsigned short*)((char*)d_ws + 16384);
  float* part = (float*)((char*)d_ws + 65536);
  k_qproj<<<256, 256, 0, stream>>>(x, wq, q);
  k_attn<<<256, NW * 64, 0, stream>>>(q, kc, vc, part);
  k_combine<<<HQ, 128, 0, stream>>>(part, o);
  k_oproj<<<64, 256, 0, stream>>>(o, wo, (unsigned short*)d_out);
}
```

```cpp
#include <hip/hip_runtime.h>
#include <hip/hip_bf16.h>
#include <stdint.h>

typedef __attribute__((ext_vector_type(8))) __bf16 bf16x8;
typedef __attribute__((ext_vector_type(2))) __bf16 bf16x2;
typedef __attribute__((ext_vector_type(4))) float f32x4;
typedef __attribute__((ext_vector_type(2))) float f32x2;
typedef __attribute__((ext_vector_type(4))) unsigned u32x4;
typedef __attribute__((ext_vector_type(2))) unsigned u32x2;

#define NCB 64
#define KVMODE 0

constexpr int DM = 512, HQ = 32, HKV = 8, SK = 65536, DH = 128, G = 4;
constexpr int NSPLIT = 32, NW = 8, NWG = 256;
constexpr int NB = SK / NSPLIT / (32 * NW);
constexpr float C2 = 0.08838834764831845f * 1.4426950408889634f;
constexpr unsigned WS_QG = 4096, WS_PA = 65536, WS_PB = WS_PA + 2097152, WS_END = WS_PB + 1048576;
constexpr unsigned SPIN_CAP = 1u << 18;
constexpr int L_WO = 0;
constexpr int L_MRG = 16384;
constexpr int L_Q = L_MRG + 16896;
constexpr int L_RED = L_Q + 1024;
constexpr int L_REC = L_RED + 512;
constexpr int L_FIN = L_REC + 640;
constexpr int L_TOTAL = 96 * 1024;

__device__ __forceinline__ unsigned cvtpk(float lo, float hi) {
  bf16x2 r = {(__bf16)lo, (__bf16)hi};
  return __builtin_bit_cast(unsigned, r);
}
__device__ __forceinline__ float bflo(unsigned w) { return __builtin_bit_cast(float, w << 16); }
__device__ __forceinline__ float bfhi(unsigned w) { return __builtin_bit_cast(float, w & 0xffff0000u); }
template <int CTRL> __device__ __forceinline__ float dpp(float x) {
  return __builtin_bit_cast(float, __builtin_amdgcn_mov_dpp(__builtin_bit_cast(int, x), CTRL, 0xf, 0xf, true));
}
__device__ __forceinline__ float xg_max(float x) {
  auto s = __builtin_amdgcn_permlane16_swap(__float_as_uint(x), __float_as_uint(x), false, false);
  x = fmaxf(__uint_as_float(s[0]), __uint_as_float(s[1]));
  auto t = __builtin_amdgcn_permlane32_swap(__float_as_uint(x), __float_as_uint(x), false, false);
  return fmaxf(__uint_as_float(t[0]), __uint_as_float(t[1]));
}
__device__ __forceinline__ float xg_sum(float x) {
  auto s = __builtin_amdgcn_permlane16_swap(__float_as_uint(x), __float_as_uint(x), false, false);
  x = __uint_as_float(s[0]) + __uint_as_float(s[1]);
  auto t = __builtin_amdgcn_permlane32_swap(__float_as_uint(x), __float_as_uint(x), false, false);
  return __uint_as_float(t[0]) + __uint_as_float(t[1]);
}
__device__ __forceinline__ void wg_barrier() {
  asm volatile("s_waitcnt lgkmcnt(0)\n\ts_barrier" ::: "memory");
}
#define STAMP(i)

__global__ void __launch_bounds__(NW * 64, 2)
gqa_decode_fused(const unsigned short* __restrict__ x, const unsigned short* __restrict__ kc, const unsigned short* __restrict__ vc,
                 const unsigned short* __restrict__ wq, const unsigned short* __restrict__ wo, unsigned* __restrict__ out,
                 unsigned char* __restrict__ ws) {
  extern __shared__ __attribute__((aligned(16))) unsigned char lds[];
  const int tid = threadIdx.x, lane = tid & 63, wave = __builtin_amdgcn_readfirstlane(tid >> 6);
  const int bid = blockIdx.x, h = bid & 7, sp = bid >> 3;
  const int r = lane & 15, g = lane >> 4;
  STAMP(0);
  const __amdgpu_buffer_rsrc_t wsr = __builtin_amdgcn_make_buffer_rsrc(ws, 0, WS_END, 0x00020000);
  const size_t hbase = (size_t)h * SK * DH;
  const unsigned short* kb = kc + hbase + (size_t)(4 * (r & 3) + (r >> 2)) * DH + g * 8;
  const unsigned short* vb = vc + hbase + lane * 8;
  u32x4 kf[2][8], vf[2][8];
  auto blkoff = [&](int b) -> size_t {
    const int blk = KVMODE == 0 ? sp * (NB * NW) + b * NW + wave : (b * NSPLIT + sp) * NW + wave;
    return (size_t)blk * 32 * DH;
  };
  auto loadK = [&](int b) {
    const size_t off = blkoff(b);
#pragma unroll
    for (int t = 0; t < 2; ++t)
#pragma unroll
      for (int s = 0; s < 4; ++s) kf[b & 1][t * 4 + s] = __builtin_nontemporal_load((const u32x4*)(kb + off + t * 16 * DH + s * 32));
  };
  auto loadV = [&](int b) {
    const size_t off = blkoff(b);
#pragma unroll
    for (int j = 0; j < 8; ++j) vf[b & 1][j] = __builtin_nontemporal_load((const u32x4*)(vb + off + j * 4 * DH));
  };
  const unsigned epoch_v = __builtin_amdgcn_raw_buffer_load_b32(wsr, 0, 0, 16);
  if (wave == 0) {
    __builtin_amdgcn_s_setprio(3);
    const int rg = lane >> 1;
    const unsigned short* wqp = wq + (size_t)(rg * 16) * (HQ * DH) + h * 512 + sp * 16 + (lane & 1) * 8;
    const u32x4 xa = *(const u32x4*)(x + rg * 16), xb = *(const u32x4*)(x + rg * 16 + 8);
    u32x4 wv[16];
#pragma unroll
    for (int i = 0; i < 16; ++i) wv[i] = *(const u32x4*)(wqp + (size_t)i * (HQ * DH));
    __builtin_amdgcn_sched_barrier(0);
    float a[8];
#pragma unroll
    for (int k = 0; k < 8; ++k) a[k] = 0.f;
#pragma unroll
    for (int i = 0; i < 8; ++i) {
      const bf16x2 xp = __builtin_bit_cast(bf16x2, i < 4 ? xa[i & 3] : xb[i & 3]);
#pragma unroll
      for (int k = 0; k < 4; ++k) {
        const unsigned lo = __builtin_amdgcn_perm(wv[2 * i + 1][k], wv[2 * i][k], 0x05040100u);
        const unsigned hi = __builtin_amdgcn_perm(wv[2 * i + 1][k], wv[2 * i][k], 0x07060302u);
        a[2 * k] = __builtin_amdgcn_fdot2_f32_bf16(__builtin_bit_cast(bf16x2, lo), xp, a[2 * k], false);
        a[2 * k + 1] = __builtin_amdgcn_fdot2_f32_bf16(__builtin_bit_cast(bf16x2, hi), xp, a[2 * k + 1], false);
      }
    }
    float b4[4], b2[2];
#pragma unroll
    for (int k = 0; k < 4; ++k) {
      auto s = __builtin_amdgcn_permlane32_swap(__float_as_uint(a[k]), __float_as_uint(a[k + 4]), false, false);
      b4[k] = __uint_as_float(s[0]) + __uint_as_float(s[1]);
    }
#pragma unroll
    for (int k = 0; k < 2; ++k) {
      auto s = __builtin_amdgcn_permlane16_swap(__float_as_uint(b4[k]), __float_as_uint(b4[k + 2]), false, false);
      b2[k] = __uint_as_float(s[0]) + __uint_as_float(s[1]);
    }
#pragma unroll
    for (int k = 0; k < 2; ++k) {
      b2[k] += dpp<0x128>(b2[k]);
      b2[k] += dpp<0x124>(b2[k]);
      b2[k] += dpp<0x122>(b2[k]);
    }
    const unsigned tg = __builtin_amdgcn_readfirstlane(epoch_v) + 1u;
    if (r < 2) {
      const u32x2 gr = {cvtpk(b2[0], b2[1]), tg};
      __builtin_amdgcn_raw_buffer_store_b64(gr, wsr, WS_QG + h * 2048 + sp * 64 + r * 32 + g * 8, 0, 16);
    }
    STAMP(1);
    u32x4 qp0, qp1;
    unsigned spins = 0;
    do {
      asm volatile("" ::: "memory");
      qp0 = __builtin_amdgcn_raw_buffer_load_b128(wsr, WS_QG + h * 2048 + lane * 32, 0, 16);
      qp1 = __builtin_amdgcn_raw_buffer_load_b128(wsr, WS_QG + h * 2048 + lane * 32 + 16, 0, 16);
    } while (!__all(qp0[1] == tg && qp0[3] == tg && qp1[1] == tg && qp1[3] == tg) && ++spins < SPIN_CAP);
    *(u32x4*)(lds + L_Q + lane * 16) = u32x4{qp0[0], qp0[2], qp1[0], qp1[2]};
    STAMP(2);
    __builtin_amdgcn_s_setprio(0);
  }
  __builtin_amdgcn_sched_barrier(0);
  const int jrow = (h * G * DH) + sp * 16;
  const unsigned short* wop = wo + (size_t)(jrow + 2 * wave) * DM + lane * 8;
  const u32x4 wo0 = __builtin_nontemporal_load((const u32x4*)wop), wo1 = __builtin_nontemporal_load((const u32x4*)(wop + DM));
  loadK(0);
  loadV(0);
  loadK(1);
  loadV(1);
  __builtin_amdgcn_sched_barrier(0);
  STAMP(3);
  const unsigned tag = __builtin_amdgcn_readfirstlane(epoch_v) + 1u;
  *(u32x4*)(lds + L_WO + (2 * wave) * 1024 + lane * 16) = wo0;
  *(u32x4*)(lds + L_WO + (2 * wave + 1) * 1024 + lane * 16) = wo1;
  wg_barrier();
  u32x4 qf[4];
#pragma unroll
  for (int s = 0; s < 4; ++s) {
    const u32x4 t = *(const u32x4*)(lds + L_Q + (r & 3) * 256 + (4 * s + g) * 16);
    qf[s] = r < G ? t : u32x4{0u, 0u, 0u, 0u};
  }
  STAMP(4);
  float m = -INFINITY, l = 0.f;
  f32x4 acc[8];
#pragma unroll
  for (int d = 0; d < 8; ++d) acc[d] = f32x4{0.f, 0.f, 0.f, 0.f};
#pragma unroll
  for (int b = 0; b < NB; ++b) {
    f32x4 s0 = {0.f, 0.f, 0.f, 0.f}, s1 = {0.f, 0.f, 0.f, 0.f};
#pragma unroll
    for (int s = 0; s < 4; ++s) {
      s0 = __builtin_amdgcn_mfma_f32_16x16x32_bf16(__builtin_bit_cast(bf16x8, kf[b & 1][s]), __builtin_bit_cast(bf16x8, qf[s]), s0, 0, 0, 0);
      s1 = __builtin_amdgcn_mfma_f32_16x16x32_bf16(__builtin_bit_cast(bf16x8, kf[b & 1][4 + s]), __builtin_bit_cast(bf16x8, qf[s]), s1, 0, 0, 0);
    }
    __builtin_amdgcn_sched_barrier(0);
    if (b + 2 < NB) loadK(b + 2);
    __builtin_amdgcn_sched_barrier(0);
    float mx = fmaxf(fmaxf(fmaxf(s0[0], s0[1]), fmaxf(s0[2], s0[3])), fmaxf(fmaxf(s1[0], s1[1]), fmaxf(s1[2], s1[3])));
    mx = xg_max(mx);
    if (__any(mx > m)) {
      const float mn = fmaxf(m, mx);
      const float alpha = __builtin_amdgcn_exp2f((m - mn) * C2);
      l *= alpha;
#pragma unroll
      for (int d = 0; d < 8; ++d) acc[d] *= alpha;
      m = mn;
    }
    const float mc = m * C2;
    float p[8];
#pragma unroll
    for (int i = 0; i < 4; ++i) {
      p[i] = __builtin_amdgcn_exp2f(fmaf(s0[i], C2, -mc));
      p[4 + i] = __builtin_amdgcn_exp2f(fmaf(s1[i], C2, -mc));
    }
    l += ((p[0] + p[1]) + (p[2] + p[3])) + ((p[4] + p[5]) + (p[6] + p[7]));
    const u32x4 pf = {cvtpk(p[0], p[1]), cvtpk(p[2], p[3]), cvtpk(p[4], p[5]), cvtpk(p[6], p[7])};
#pragma unroll
    for (int d = 0; d < 8; ++d) {
      const unsigned sel = (d & 1) ? 0x07060302u : 0x05040100u;
      u32x4 a;
#pragma unroll
      for (int u = 0; u < 4; ++u) a[u] = __builtin_amdgcn_perm(vf[b & 1][2 * u + 1][d >> 1], vf[b & 1][2 * u][d >> 1], sel);
      acc[d] = __builtin_amdgcn_mfma_f32_16x16x32_bf16(__builtin_bit_cast(bf16x8, a), __builtin_bit_cast(bf16x8, pf), acc[d], 0, 0, 0);
    }
    __builtin_amdgcn_sched_barrier(0);
    if (b + 2 < NB) loadV(b + 2);
    __builtin_amdgcn_sched_barrier(0);
  }
  STAMP(5);
  l = xg_sum(l);
  if (r < G) {
    float* rec = (float*)(lds + L_MRG) + (wave * G + r) * 132;
#pragma unroll
    for (int i = 0; i < 4; ++i) {
      *(f32x4*)(rec + 4 + 8 * (4 * g + i)) = f32x4{acc[0][i], acc[1][i], acc[2][i], acc[3][i]};
      *(f32x4*)(rec + 4 + 8 * (4 * g + i) + 4) = f32x4{acc[4][i], acc[5][i], acc[6][i], acc[7][i]};
    }
    if (g == 0) *(f32x2*)rec = f32x2{m, l};
  }
  wg_barrier();
  STAMP(6);
  {
    const int hh = tid >> 7, dd = tid & 127;
    const float* mrg = (const float*)(lds + L_MRG) + hh * 132;
    f32x2 ml[NW];
    float ov[NW];
#pragma unroll
    for (int w = 0; w < NW; ++w) { ml[w] = *(const f32x2*)(mrg + w * G * 132); ov[w] = mrg[w * G * 132 + 4 + dd]; }
    float M = ml[0][0];
#pragma unroll
    for (int w = 1; w < NW; ++w) M = fmaxf(M, ml[w][0]);
    float L = 0.f, o = 0.f;
#pragma unroll
    for (int w = 0; w < NW; ++w) {
      const float wt = __builtin_amdgcn_exp2f((ml[w][0] - M) * C2);
      L = fmaf(wt, ml[w][1], L);
      o = fmaf(wt, ov[w], o);
    }
    const float accn = o * __builtin_amdgcn_rcpf(L), lse = fmaf(M, C2, __builtin_amdgcn_logf(L));
    const u32x4 gr = {__float_as_uint(accn), tag, __float_as_uint(lse), tag};
    __builtin_amdgcn_raw_buffer_store_b128(gr, wsr, WS_PA + ((h * 32 + hh * 8 + (dd >> 4)) * 32 + sp) * 256 + (dd & 15) * 16, 0, 16);
  }
  STAMP(7);
  unsigned wr[16];
  if (wave < 4) {
#pragma unroll
    for (int i = 0; i < 16; ++i) wr[i] = *(const unsigned*)(lds + L_WO + i * 1024 + tid * 4);
  }
  {
    u32x4 pa;
    unsigned spins = 0;
    do {
      asm volatile("" ::: "memory");
      pa = __builtin_amdgcn_raw_buffer_load_b128(wsr, WS_PA + (h * 32 + sp) * 8192 + tid * 16, 0, 16);
    } while (!__all(pa[1] == tag && pa[3] == tag) && ++spins < SPIN_CAP);
    const float accn = __uint_as_float(pa[0]), lse = __uint_as_float(pa[2]);
    const float mw = xg_max(lse);
    const float e = __builtin_amdgcn_exp2f(lse - mw);
    const float sw = xg_sum(e), ow = xg_sum(e * accn);
    float* rec = (float*)(lds + L_REC) + wave * 20;
    if (g == 0) rec[4 + r] = ow;
    if (lane == 0) *(f32x2*)rec = f32x2{mw, sw};
  }
  wg_barrier();
  STAMP(8);
  if (wave < 4) {
    const float* rec = (const float*)(lds + L_REC);
    f32x2 ms[NW];
    float ov[NW];
#pragma unroll
    for (int w = 0; w < NW; ++w) { ms[w] = *(const f32x2*)(rec + w * 20); ov[w] = rec[w * 20 + 4 + r]; }
    float M = ms[0][0];
#pragma unroll
    for (int w = 1; w < NW; ++w) M = fmaxf(M, ms[w][0]);
    float S = 0.f, o = 0.f;
#pragma unroll
    for (int w = 0; w < NW; ++w) {
      const float wt = __builtin_amdgcn_exp2f(ms[w][0] - M);
      S = fmaf(wt, ms[w][1], S);
      o = fmaf(wt, ov[w], o);
    }
    const unsigned ob = cvtpk(o / S, 0.f) << 16;
    float v0 = 0.f, v1 = 0.f;
#pragma unroll
    for (int i = 0; i < 16; ++i) {
      const float oi = __uint_as_float(__builtin_amdgcn_readlane(ob, i));
      v0 = fmaf(oi, bflo(wr[i]), v0);
      v1 = fmaf(oi, bfhi(wr[i]), v1);
    }
    const u32x4 gr = {__float_as_uint(v0), tag, __float_as_uint(v1), tag};
    __builtin_amdgcn_raw_buffer_store_b128(gr, wsr, WS_PB + bid * 4096 + tid * 16, 0, 16);
  }
  STAMP(9);
  if (bid < NCB) {
    constexpr int LP = 256 / NCB;
    constexpr int NL = LP >= 2 ? LP / 2 : 1;
    const int rr = tid & (LP - 1), p0 = tid / LP;
    float v0 = 0.f, v1 = 0.f;
    if (LP >= 2 || wave < 4) {
      u32x4 pb[NL];
      unsigned spins = 0;
      bool ok;
      do {
        asm volatile("" ::: "memory");
        ok = true;
#pragma unroll
        for (int k = 0; k < NL; ++k) pb[k] = __builtin_amdgcn_raw_buffer_load_b128(wsr, WS_PB + (p0 + k * (512 / LP)) * 4096 + (bid * LP + rr) * 16, 0, 16);
#pragma unroll
        for (int k = 0; k < NL; ++k) ok &= (pb[k][1] == tag && pb[k][3] == tag);
      } while (!__all(ok) && ++spins < SPIN_CAP);
#pragma unroll
      for (int k = 0; k < NL; ++k) { v0 += __uint_as_float(pb[k][0]); v1 += __uint_as_float(pb[k][2]); }
    }
    v0 = xg_sum(v0); v1 = xg_sum(v1);
    if (LP <= 8) { v0 += dpp<0x128>(v0); v1 += dpp<0x128>(v1); }
    if (LP <= 4) { v0 += dpp<0x124>(v0); v1 += dpp<0x124>(v1); }
    if (LP <= 2) { v0 += dpp<0x122>(v0); v1 += dpp<0x122>(v1); }
    if (LP <= 1) { v0 += dpp<0x121>(v0); v1 += dpp<0x121>(v1); }
    if (lane < LP) *(f32x2*)(lds + L_FIN + (wave * 8 + lane) * 8) = f32x2{v0, v1};
    wg_barrier();
    if (wave == 0) {
      if (lane < LP) {
        f32x2 s = *(const f32x2*)(lds + L_FIN + lane * 8);
#pragma unroll
        for (int w = 1; w < NW; ++w) s += *(const f32x2*)(lds + L_FIN + (w * 8 + lane) * 8);
        out[bid * LP + lane] = cvtpk(s[0], s[1]);
      }
      if (bid == 0 && lane == 0) __builtin_amdgcn_raw_buffer_store_b32(tag, wsr, 0, 0, 16);
    }
  }
  STAMP(10);
}

extern "C" void kernel_launch(void* const* d_in, const int* in_sizes, int n_in, void* d_out, int out_size, void* d_ws,
                              size_t ws_size, hipStream_t stream) {
  (void)hipFuncSetAttribute((const void*)gqa_decode_fused, hipFuncAttributeMaxDynamicSharedMemorySize, L_TOTAL);
  gqa_decode_fused<<<NWG, NW * 64, L_TOTAL, stream>>>((const unsigned short*)d_in[0], (const unsigned short*)d_in[1],
                                                      (const unsigned short*)d_in[2], (const unsigned short*)d_in[3],
                                                      (const unsigned short*)d_in[4], (unsigned*)d_out, (unsigned char*)d_ws);
}
```

```cpp
#define DIAG 0
#include <hip/hip_runtime.h>
#include <hip/hip_bf16.h>
#include <stdint.h>

typedef __attribute__((ext_vector_type(8))) __bf16 bf16x8;
typedef __attribute__((ext_vector_type(2))) __bf16 bf16x2;
typedef __attribute__((ext_vector_type(4))) float f32x4;
typedef __attribute__((ext_vector_type(2))) float f32x2;
typedef __attribute__((ext_vector_type(4))) unsigned u32x4;
typedef __attribute__((ext_vector_type(2))) unsigned u32x2;

#define NCB 64
#define KVMODE 0

constexpr int DM = 512, HQ = 32, HKV = 8, SK = 65536, DH = 128, G = 4;
constexpr int NSPLIT = 32, NW = 8, NWG = 256;
constexpr int NB = SK / NSPLIT / (32 * NW);
constexpr float C2 = 0.08838834764831845f * 1.4426950408889634f;
constexpr unsigned WS_QG = 4096, WS_PA = 65536, WS_PB = WS_PA + 2097152, WS_END = WS_PB + 1048576;
constexpr unsigned SPIN_CAP = 1u << 18;
constexpr int L_WO = 0;
constexpr int L_MRG = 16384;
constexpr int L_Q = L_MRG + 16896;
constexpr int L_RED = L_Q + 1024;
constexpr int L_REC = L_RED + 512;
constexpr int L_FIN = L_REC + 640;
constexpr int L_TOTAL = 96 * 1024;

__device__ __forceinline__ unsigned cvtpk(float lo, float hi) {
  bf16x2 r = {(__bf16)lo, (__bf16)hi};
  return __builtin_bit_cast(unsigned, r);
}
__device__ __forceinline__ float bflo(unsigned w) { return __builtin_bit_cast(float, w << 16); }
__device__ __forceinline__ float bfhi(unsigned w) { return __builtin_bit_cast(float, w & 0xffff0000u); }
template <int CTRL> __device__ __forceinline__ float dpp(float x) {
  return __builtin_bit_cast(float, __builtin_amdgcn_mov_dpp(__builtin_bit_cast(int, x), CTRL, 0xf, 0xf, true));
}
__device__ __forceinline__ float xg_max(float x) {
  auto s = __builtin_amdgcn_permlane16_swap(__float_as_uint(x), __float_as_uint(x), false, false);
  x = fmaxf(__uint_as_float(s[0]), __uint_as_float(s[1]));
  auto t = __builtin_amdgcn_permlane32_swap(__float_as_uint(x), __float_as_uint(x), false, false);
  return fmaxf(__uint_as_float(t[0]), __uint_as_float(t[1]));
}
__device__ __forceinline__ float xg_sum(float x) {
  auto s = __builtin_amdgcn_permlane16_swap(__float_as_uint(x), __float_as_uint(x), false, false);
  x = __uint_as_float(s[0]) + __uint_as_float(s[1]);
  auto t = __builtin_amdgcn_permlane32_swap(__float_as_uint(x), __float_as_uint(x), false, false);
  return __uint_as_float(t[0]) + __uint_as_float(t[1]);
}
__device__ __forceinline__ void wg_barrier() {
  asm volatile("s_waitcnt lgkmcnt(0)\n\ts_barrier" ::: "memory");
}
#define STAMP(i) do { if ((i) == 0) dg0 = __builtin_amdgcn_s_memrealtime(); if ((i) == DIAG) dgk = __builtin_amdgcn_s_memrealtime(); } while (0)

__global__ void __launch_bounds__(NW * 64, 2)
gqa_decode_fused(const unsigned short* __restrict__ x, const unsigned short* __restrict__ kc, const unsigned short* __restrict__ vc,
                 const unsigned short* __restrict__ wq, const unsigned short* __restrict__ wo, unsigned* __restrict__ out,
                 unsigned char* __restrict__ ws) {
  extern __shared__ __attribute__((aligned(16))) unsigned char lds[];
  const int tid = threadIdx.x, lane = tid & 63, wave = __builtin_amdgcn_readfirstlane(tid >> 6);
  const int bid = blockIdx.x, h = bid & 7, sp = bid >> 3;
  const int r = lane & 15, g = lane >> 4;
  unsigned long long dg0 = 0, dgk = 0;
  STAMP(0);
  const __amdgpu_buffer_rsrc_t wsr = __builtin_amdgcn_make_buffer_rsrc(ws, 0, WS_END, 0x00020000);
  const size_t hbase = (size_t)h * SK * DH;
  const unsigned short* kb = kc + hbase + (size_t)(4 * (r & 3) + (r >> 2)) * DH + g * 8;
  const unsigned short* vb = vc + hbase + lane * 8;
  u32x4 kf[2][8], vf[2][8];
  auto blkoff = [&](int b) -> size_t {
    const int blk = KVMODE == 0 ? sp * (NB * NW) + b * NW + wave : (b * NSPLIT + sp) * NW + wave;
    return (size_t)blk * 32 * DH;
  };
  auto loadK = [&](int b) {
    const size_t off = blkoff(b);
#pragma unroll
    for (int t = 0; t < 2; ++t)
#pragma unroll
      for (int s = 0; s < 4; ++s) kf[b & 1][t * 4 + s] = __builtin_nontemporal_load((const u32x4*)(kb + off + t * 16 * DH + s * 32));
  };
  auto loadV = [&](int b) {
    const size_t off = blkoff(b);
#pragma unroll
    for (int j = 0; j < 8; ++j) vf[b & 1][j] = __builtin_nontemporal_load((const u32x4*)(vb + off + j * 4 * DH));
  };
  const unsigned epoch_v = __builtin_amdgcn_raw_buffer_load_b32(wsr, 0, 0, 16);
  if (wave == 0) {
    __builtin_amdgcn_s_setprio(3);
    const int rg = lane >> 1;
    const unsigned short* wqp = wq + (size_t)(rg * 16) * (HQ * DH) + h * 512 + sp * 16 + (lane & 1) * 8;
    const u32x4 xa = *(const u32x4*)(x + rg * 16), xb = *(const u32x4*)(x + rg * 16 + 8);
    u32x4 wv[16];
#pragma unroll
    for (int i = 0; i < 16; ++i) wv[i] = *(const u32x4*)(wqp + (size_t)i * (HQ * DH));
    __builtin_amdgcn_sched_barrier(0);
    __builtin_amdgcn_s_barrier();
    __builtin_amdgcn_sched_barrier(0);
    float a[8];
#pragma unroll
    for (int k = 0; k < 8; ++k) a[k] = 0.f;
#pragma unroll
    for (int i = 0; i < 8; ++i) {
      const bf16x2 xp = __builtin_bit_cast(bf16x2, i < 4 ? xa[i & 3] : xb[i & 3]);
#pragma unroll
      for (int k = 0; k < 4; ++k) {
        const unsigned lo = __builtin_amdgcn_perm(wv[2 * i + 1][k], wv[2 * i][k], 0x05040100u);
        const unsigned hi = __builtin_amdgcn_perm(wv[2 * i + 1][k], wv[2 * i][k], 0x07060302u);
        a[2 * k] = __builtin_amdgcn_fdot2_f32_bf16(__builtin_bit_cast(bf16x2, lo), xp, a[2 * k], false);
        a[2 * k + 1] = __builtin_amdgcn_fdot2_f32_bf16(__builtin_bit_cast(bf16x2, hi), xp, a[2 * k + 1], false);
      }
    }
    float b4[4], b2[2];
#pragma unroll
    for (int k = 0; k < 4; ++k) {
      auto s = __builtin_amdgcn_permlane32_swap(__float_as_uint(a[k]), __float_as_uint(a[k + 4]), false, false);
      b4[k] = __uint_as_float(s[0]) + __uint_as_float(s[1]);
    }
#pragma unroll
    for (int k = 0; k < 2; ++k) {
      auto s = __builtin_amdgcn_permlane16_swap(__float_as_uint(b4[k]), __float_as_uint(b4[k + 2]), false, false);
      b2[k] = __uint_as_float(s[0]) + __uint_as_float(s[1]);
    }
#pragma unroll
    for (int k = 0; k < 2; ++k) {
      b2[k] += dpp<0x128>(b2[k]);
      b2[k] += dpp<0x124>(b2[k]);
      b2[k] += dpp<0x122>(b2[k]);
    }
    const unsigned tg = __builtin_amdgcn_readfirstlane(epoch_v) + 1u;
    if (r < 2) {
      const u32x2 gr = {cvtpk(b2[0], b2[1]), tg};
      __builtin_amdgcn_raw_buffer_store_b64(gr, wsr, WS_QG + h * 2048 + sp * 64 + r * 32 + g * 8, 0, 16);
    }
    STAMP(1);
    u32x4 qp0, qp1;
    unsigned spins = 0;
    do {
      asm volatile("" ::: "memory");
      qp0 = __builtin_amdgcn_raw_buffer_load_b128(wsr, WS_QG + h * 2048 + lane * 32, 0, 16);
      qp1 = __builtin_amdgcn_raw_buffer_load_b128(wsr, WS_QG + h * 2048 + lane * 32 + 16, 0, 16);
    } while (!__all(qp0[1] == tg && qp0[3] == tg && qp1[1] == tg && qp1[3] == tg) && ++spins < SPIN_CAP);
    *(u32x4*)(lds + L_Q + lane * 16) = u32x4{qp0[0], qp0[2], qp1[0], qp1[2]};
    STAMP(2);
    __builtin_amdgcn_s_setprio(0);
  } else {
    __builtin_amdgcn_s_barrier();
  }
  __builtin_amdgcn_sched_barrier(0);
  const int jrow = (h * G * DH) + sp * 16;
  const unsigned short* wop = wo + (size_t)(jrow + 2 * wave) * DM + lane * 8;
  const u32x4 wo0 = __builtin_nontemporal_load((const u32x4*)wop), wo1 = __builtin_nontemporal_load((const u32x4*)(wop + DM));
  loadK(0);
  loadV(0);
  loadK(1);
  loadV(1);
  __builtin_amdgcn_sched_barrier(0);
  STAMP(3);
  const unsigned tag = __builtin_amdgcn_readfirstlane(epoch_v) + 1u;
  *(u32x4*)(lds + L_WO + (2 * wave) * 1024 + lane * 16) = wo0;
  *(u32x4*)(lds + L_WO + (2 * wave + 1) * 1024 + lane * 16) = wo1;
  wg_barrier();
  u32x4 qf[4];
#pragma unroll
  for (int s = 0; s < 4; ++s) {
    const u32x4 t = *(const u32x4*)(lds + L_Q + (r & 3) * 256 + (4 * s + g) * 16);
    qf[s] = r < G ? t : u32x4{0u, 0u, 0u, 0u};
  }
  STAMP(4);
  float m = -INFINITY, l = 0.f;
  f32x4 acc[8];
#pragma unroll
  for (int d = 0; d < 8; ++d) acc[d] = f32x4{0.f, 0.f, 0.f, 0.f};
#pragma unroll
  for (int b = 0; b < NB; ++b) {
    f32x4 s0 = {0.f, 0.f, 0.f, 0.f}, s1 = {0.f, 0.f, 0.f, 0.f};
#pragma unroll
    for (int s = 0; s < 4; ++s) {
      s0 = __builtin_amdgcn_mfma_f32_16x16x32_bf16(__builtin_bit_cast(bf16x8, kf[b & 1][s]), __builtin_bit_cast(bf16x8, qf[s]), s0, 0, 0, 0);
      s1 = __builtin_amdgcn_mfma_f32_16x16x32_bf16(__builtin_bit_cast(bf16x8, kf[b & 1][4 + s]), __builtin_bit_cast(bf16x8, qf[s]), s1, 0, 0, 0);
    }
    __builtin_amdgcn_sched_barrier(0);
    if (b + 2 < NB) loadK(b + 2);
    __builtin_amdgcn_sched_barrier(0);
    float mx = fmaxf(fmaxf(fmaxf(s0[0], s0[1]), fmaxf(s0[2], s0[3])), fmaxf(fmaxf(s1[0], s1[1]), fmaxf(s1[2], s1[3])));
    mx = xg_max(mx);
    if (__any(mx > m)) {
      const float mn = fmaxf(m, mx);
      const float alpha = __builtin_amdgcn_exp2f((m - mn) * C2);
      l *= alpha;
#pragma unroll
      for (int d = 0; d < 8; ++d) acc[d] *= alpha;
      m = mn;
    }
    const float mc = m * C2;
    float p[8];
#pragma unroll
    for (int i = 0; i < 4; ++i) {
      p[i] = __builtin_amdgcn_exp2f(fmaf(s0[i], C2, -mc));
      p[4 + i] = __builtin_amdgcn_exp2f(fmaf(s1[i], C2, -mc));
    }
    l += ((p[0] + p[1]) + (p[2] + p[3])) + ((p[4] + p[5]) + (p[6] + p[7]));
    const u32x4 pf = {cvtpk(p[0], p[1]), cvtpk(p[2], p[3]), cvtpk(p[4], p[5]), cvtpk(p[6], p[7])};
#pragma unroll
    for (int d = 0; d < 8; ++d) {
      const unsigned sel = (d & 1) ? 0x07060302u : 0x05040100u;
      u32x4 a;
#pragma unroll
      for (int u = 0; u < 4; ++u) a[u] = __builtin_amdgcn_perm(vf[b & 1][2 * u + 1][d >> 1], vf[b & 1][2 * u][d >> 1], sel);
      acc[d] = __builtin_amdgcn_mfma_f32_16x16x32_bf16(__builtin_bit_cast(bf16x8, a), __builtin_bit_cast(bf16x8, pf), acc[d], 0, 0, 0);
    }
    __builtin_amdgcn_sched_barrier(0);
    if (b + 2 < NB) loadV(b + 2);
    __builtin_amdgcn_sched_barrier(0);
  }
  STAMP(5);
  l = xg_sum(l);
  if (r < G) {
    float* rec = (float*)(lds + L_MRG) + (wave * G + r) * 132;
#pragma unroll
    for (int i = 0; i < 4; ++i) {
      *(f32x4*)(rec + 4 + 8 * (4 * g + i)) = f32x4{acc[0][i], acc[1][i], acc[2][i], acc[3][i]};
      *(f32x4*)(rec + 4 + 8 * (4 * g + i) + 4) = f32x4{acc[4][i], acc[5][i], acc[6][i], acc[7][i]};
    }
    if (g == 0) *(f32x2*)rec = f32x2{m, l};
  }
  wg_barrier();
  STAMP(6);
  {
    const int hh = tid >> 7, dd = tid & 127;
    const float* mrg = (const float*)(lds + L_MRG) + hh * 132;
    f32x2 ml[NW];
    float ov[NW];
#pragma unroll
    for (int w = 0; w < NW; ++w) { ml[w] = *(const f32x2*)(mrg + w * G * 132); ov[w] = mrg[w * G * 132 + 4 + dd]; }
    float M = ml[0][0];
#pragma unroll
    for (int w = 1; w < NW; ++w) M = fmaxf(M, ml[w][0]);
    float L = 0.f, o = 0.f;
#pragma unroll
    for (int w = 0; w < NW; ++w) {
      const float wt = __builtin_amdgcn_exp2f((ml[w][0] - M) * C2);
      L = fmaf(wt, ml[w][1], L);
      o = fmaf(wt, ov[w], o);
    }
    const float accn = o * __builtin_amdgcn_rcpf(L), lse = fmaf(M, C2, __builtin_amdgcn_logf(L));
    const u32x4 gr = {__float_as_uint(accn), tag, __float_as_uint(lse), tag};
    __builtin_amdgcn_raw_buffer_store_b128(gr, wsr, WS_PA + ((h * 32 + hh * 8 + (dd >> 4)) * 32 + sp) * 256 + (dd & 15) * 16, 0, 16);
  }
  STAMP(7);
  unsigned wr[16];
  if (wave < 4) {
#pragma unroll
    for (int i = 0; i < 16; ++i) wr[i] = *(const unsigned*)(lds + L_WO + i * 1024 + tid * 4);
  }
  {
    u32x4 pa;
    unsigned spins = 0;
    do {
      asm volatile("" ::: "memory");
      pa = __builtin_amdgcn_raw_buffer_load_b128(wsr, WS_PA + (h * 32 + sp) * 8192 + tid * 16, 0, 16);
    } while (!__all(pa[1] == tag && pa[3] == tag) && ++spins < SPIN_CAP);
    const float accn = __uint_as_float(pa[0]), lse = __uint_as_float(pa[2]);
    const float mw = xg_max(lse);
    const float e = __builtin_amdgcn_exp2f(lse - mw);
    const float sw = xg_sum(e), ow = xg_sum(e * accn);
    float* rec = (float*)(lds + L_REC) + wave * 20;
    if (g == 0) rec[4 + r] = ow;
    if (lane == 0) *(f32x2*)rec = f32x2{mw, sw};
  }
  wg_barrier();
  STAMP(8);
  if (wave < 4) {
    const float* rec = (const float*)(lds + L_REC);
    f32x2 ms[NW];
    float ov[NW];
#pragma unroll
    for (int w = 0; w < NW; ++w) { ms[w] = *(const f32x2*)(rec + w * 20); ov[w] = rec[w * 20 + 4 + r]; }
    float M = ms[0][0];
#pragma unroll
    for (int w = 1; w < NW; ++w) M = fmaxf(M, ms[w][0]);
    float S = 0.f, o = 0.f;
#pragma unroll
    for (int w = 0; w < NW; ++w) {
      const float wt = __builtin_amdgcn_exp2f(ms[w][0] - M);
      S = fmaf(wt, ms[w][1], S);
      o = fmaf(wt, ov[w], o);
    }
    const unsigned ob = cvtpk(o / S, 0.f) << 16;
    float v0 = 0.f, v1 = 0.f;
#pragma unroll
    for (int i = 0; i < 16; ++i) {
      const float oi = __uint_as_float(__builtin_amdgcn_readlane(ob, i));
      v0 = fmaf(oi, bflo(wr[i]), v0);
      v1 = fmaf(oi, bfhi(wr[i]), v1);
    }
    const u32x4 gr = {__float_as_uint(v0), tag, __float_as_uint(v1), tag};
    __builtin_amdgcn_raw_buffer_store_b128(gr, wsr, WS_PB + bid * 4096 + tid * 16, 0, 16);
  }
  STAMP(9);
  if (bid < NCB) {
    constexpr int LP = 256 / NCB;
    constexpr int NL = LP >= 2 ? LP / 2 : 1;
    const int rr = tid & (LP - 1), p0 = tid / LP;
    float v0 = 0.f, v1 = 0.f;
    if (LP >= 2 || wave < 4) {
      u32x4 pb[NL];
      unsigned spins = 0;
      bool ok;
      do {
        asm volatile("" ::: "memory");
        ok = true;
#pragma unroll
        for (int k = 0; k < NL; ++k) pb[k] = __builtin_amdgcn_raw_buffer_load_b128(wsr, WS_PB + (p0 + k * (512 / LP)) * 4096 + (bid * LP + rr) * 16, 0, 16);
#pragma unroll
        for (int k = 0; k < NL; ++k) ok &= (pb[k][1] == tag && pb[k][3] == tag);
      } while (!__all(ok) && ++spins < SPIN_CAP);
#pragma unroll
      for (int k = 0; k < NL; ++k) { v0 += __uint_as_float(pb[k][0]); v1 += __uint_as_float(pb[k][2]); }
    }
    v0 = xg_sum(v0); v1 = xg_sum(v1);
    if (LP <= 8) { v0 += dpp<0x128>(v0); v1 += dpp<0x128>(v1); }
    if (LP <= 4) { v0 += dpp<0x124>(v0); v1 += dpp<0x124>(v1); }
    if (LP <= 2) { v0 += dpp<0x122>(v0); v1 += dpp<0x122>(v1); }
    if (LP <= 1) { v0 += dpp<0x121>(v0); v1 += dpp<0x121>(v1); }
    if (lane < LP) *(f32x2*)(lds + L_FIN + (wave * 8 + lane) * 8) = f32x2{v0, v1};
    wg_barrier();
    if (wave == 0) {
      if (lane < LP) {
        f32x2 s = *(const f32x2*)(lds + L_FIN + lane * 8);
#pragma unroll
        for (int w = 1; w < NW; ++w) s += *(const f32x2*)(lds + L_FIN + (w * 8 + lane) * 8);
        out[bid * LP + lane] = cvtpk(s[0], s[1]);
      }
      if (bid == 0 && lane == 0) __builtin_amdgcn_raw_buffer_store_b32(tag, wsr, 0, 0, 16);
    }
  }
  STAMP(10);
  if (wave == 0) { const unsigned long long until = dgk + 10000ull; while (__builtin_amdgcn_s_memrealtime() < until) __builtin_amdgcn_s_sleep(2); }
}

extern "C" void kernel_launch(void* const* d_in, const int* in_sizes, int n_in, void* d_out, int out_size, void* d_ws,
                              size_t ws_size, hipStream_t stream) {
  (void)hipFuncSetAttribute((const void*)gqa_decode_fused, hipFuncAttributeMaxDynamicSharedMemorySize, L_TOTAL);
  gqa_decode_fused<<<NWG, NW * 64, L_TOTAL, stream>>>((const unsigned short*)d_in[0], (const unsigned short*)d_in[1],
                                                      (const unsigned short*)d_in[2], (const unsigned short*)d_in[3],
                                                      (const unsigned short*)d_in[4], (unsigned*)d_out, (unsigned char*)d_ws);
}
```

```cpp
#include <hip/hip_runtime.h>
#include <hip/hip_bf16.h>
#include <stdint.h>

typedef __attribute__((ext_vector_type(8))) __bf16 bf16x8;
typedef __attribute__((ext_vector_type(2))) __bf16 bf16x2;
typedef __attribute__((ext_vector_type(4))) float f32x4;
typedef __attribute__((ext_vector_type(2))) float f32x2;
typedef __attribute__((ext_vector_type(4))) unsigned u32x4;
typedef __attribute__((ext_vector_type(2))) unsigned u32x2;
typedef __attribute__((ext_vector_type(4))) short s16x4;
#define LAS __attribute__((address_space(3)))

constexpr int DM = 512, NW = 9, NWG = 256, NSLOT = 8, NS = 64;
constexpr float C2 = 0.08838834764831845f * 1.4426950408889634f;
constexpr unsigned WS_TS = 1024, WS_LS = 4096, WS_QG = 16384, WS_PB = 131072, WS_PA = WS_PB + 262144, WS_END = WS_PA + 524288;
constexpr unsigned OOB = 0x80000000u, SPIN_CAP = 1u << 18;
constexpr int SC1 = 16, MARK = 34;
constexpr int RING = NSLOT * 16384;
constexpr int L_P = RING;
constexpr int L_M = L_P + NS * 256;
constexpr int L_FT = L_M + NS * 16;
constexpr int L_PROG = L_FT + 128;
constexpr int L_QF = L_PROG + 64;
constexpr int L_ML = L_QF + 16;
constexpr int L_SC = L_ML + 128;
constexpr int L_X = L_SC + 16;
constexpr int L_Q = L_X + 256;
constexpr int L_TOTAL = L_Q + 1024;
static_assert(L_TOTAL <= 160 * 1024 && L_TOTAL > 80 * 1024, "one workgroup per CU");

__device__ __forceinline__ unsigned cvtpk(float lo, float hi) {
  bf16x2 r = {(__bf16)lo, (__bf16)hi};
  return __builtin_bit_cast(unsigned, r);
}
template <int CTRL> __device__ __forceinline__ float dpp(float x) {
  return __builtin_bit_cast(float, __builtin_amdgcn_mov_dpp(__builtin_bit_cast(int, x), CTRL, 0xf, 0xf, true));
}
__device__ __forceinline__ float fmx(float a, float b) { return __builtin_elementwise_maximum(a, b); }
__device__ __forceinline__ float swap32_add(float a, float b) {
  auto s = __builtin_amdgcn_permlane32_swap(__float_as_uint(a), __float_as_uint(b), false, false);
  return __uint_as_float(s[0]) + __uint_as_float(s[1]);
}
__device__ __forceinline__ float swap16_add(float a, float b) {
  auto s = __builtin_amdgcn_permlane16_swap(__float_as_uint(a), __float_as_uint(b), false, false);
  return __uint_as_float(s[0]) + __uint_as_float(s[1]);
}
__device__ __forceinline__ float xg_max(float x, float floor_) {
  auto s = __builtin_amdgcn_permlane16_swap(__float_as_uint(x), __float_as_uint(x), false, false);
  x = fmx(__uint_as_float(s[0]), __uint_as_float(s[1]));
  auto t = __builtin_amdgcn_permlane32_swap(__float_as_uint(x), __float_as_uint(x), false, false);
  return fmx(fmx(__uint_as_float(t[0]), __uint_as_float(t[1])), floor_);
}
template <int SLEEP> __device__ __forceinline__ int lds_wait_ge(unsigned addr, int need) {
  int s; unsigned v, cap;
  asm volatile(
      "s_mov_b32 %[cap], 0x400000\n\t"
      "L_w%=:\n\t"
      "ds_read_b32 %[v], %[a]\n\t"
      "s_waitcnt lgkmcnt(0)\n\t"
      "v_readfirstlane_b32 %[s], %[v]\n\t"
      "s_cmp_ge_i32 %[s], %[need]\n\t"
      "s_cbranch_scc1 L_d%=\n\t"
      ".if %c[sl] > 0\n\t"
      "s_sleep %c[sl]\n\t"
      ".endif\n\t"
      "s_sub_u32 %[cap], %[cap], 1\n\t"
      "s_cbranch_scc0 L_w%=\n\t"
      "L_d%=:\n\t"
      : [s] "=&s"(s), [v] "=&v"(v), [cap] "=&s"(cap) : [a] "v"(addr), [need] "s"(need), [sl] "i"(SLEEP) : "memory", "scc");
  return s;
}
__device__ __forceinline__ u32x4 pack_side(f32x4 v, unsigned side) {
  const u32x4 a = __builtin_bit_cast(u32x4, v);
  return u32x4{__builtin_amdgcn_perm(a[0], side, 0x07060500u), __builtin_amdgcn_perm(a[1], side, 0x07060501u),
               __builtin_amdgcn_perm(a[2], side, 0x07060502u), __builtin_amdgcn_perm(a[3], side, 0x07060503u)};
}
__device__ __forceinline__ unsigned strip_side(u32x4& d, unsigned side) {
  d ^= u32x4{side & 0xffu, (side >> 8) & 0xffu, (side >> 16) & 0xffu, side >> 24};
  return d[0] | d[1] | d[2] | d[3];
}
__device__ __forceinline__ bool pair_bad(unsigned x) {
  return ((x | (unsigned)__builtin_amdgcn_mov_dpp((int)x, 0xB1, 0xf, 0xf, true)) & 0xffu) != 0u;
}
__device__ __forceinline__ unsigned load_tag(const unsigned char* ws) {
  unsigned e;
  asm volatile("s_load_dword %0, %1, 0x0\n\ts_waitcnt lgkmcnt(0)" : "=s"(e) : "s"(ws) : "memory");
  return ((e & 0x7f7f7f7fu) + 0x01010101u) ^ (e & 0x80808080u);
}
__device__ __forceinline__ bf16x8 as_frag(u32x4 v) { return __builtin_bit_cast(bf16x8, v); }
__device__ __forceinline__ u32x4 read_tr(LAS unsigned char* p) {
  const u32x2 a = __builtin_bit_cast(u32x2, __builtin_amdgcn_ds_read_tr16_b64_v4i16((LAS s16x4*)p));
  const u32x2 b = __builtin_bit_cast(u32x2, __builtin_amdgcn_ds_read_tr16_b64_v4i16((LAS s16x4*)(p + 4096)));
  return u32x4{a[0], a[1], b[0], b[1]};
}

#define LD(w, j, voff, soff, imm)                                                          \
  "s_waitcnt vmcnt(" w ")\n\t"                                                             \
  "buffer_load_dwordx4 " voff ", s[60:63], " soff " offen offset:" imm " nt lds\n\t"       \
  ".if ((" #j " + 64 - " w ") & 7) == 0\n\t"                                               \
  "s_lshl_b32 s72, s68, 1\n\t"                                                             \
  "s_add_i32 s72, s72, (" #j " - " w ") / 8\n\t"                                           \
  "v_mov_b32 v31, s72\n\t"                                                                 \
  "ds_max_i32 v26, v31\n\t"                                                                \
  ".endif\n\t"
#define BODY(w, extra)                                                                                                     \
  LD(w, 0, "v20", "s64", "0") LD(w, 1, "v21", "s64", "1024") extra LD(w, 2, "v22", "s64", "2048") LD(w, 3, "v23", "s64", "3072") \
  "s_add_u32 m0, s69, 4096\n\t"                                                                                            \
  LD(w, 4, "v20", "s65", "0") LD(w, 5, "v21", "s65", "1024") LD(w, 6, "v22", "s65", "2048") LD(w, 7, "v23", "s65", "3072") \
  "s_add_u32 m0, s69, 8192\n\t"                                                                                            \
  LD(w, 8, "v20", "s66", "0") LD(w, 9, "v21", "s66", "1024") LD(w, 10, "v22", "s66", "2048") LD(w, 11, "v23", "s66", "3072") \
  "s_add_u32 m0, s69, 12288\n\t"                                                                                           \
  LD(w, 12, "v20", "s67", "0") LD(w, 13, "v21", "s67", "1024")                                                             \
  "ds_read_b32 v32, v27\n\t"                                                                                               \
  LD(w, 14, "v22", "s67", "2048") LD(w, 15, "v23", "s67", "3072")
#define PICK(k) "s_cmp_eq_u32 s72, " #k "\n\ts_cbranch_scc1 L_b" #k "_%=\n\t"
#define LEVEL(k, w) "L_b" #k "_%=:\n\t" BODY(w, "") "s_branch L_next%=\n\t"

__global__ void __launch_bounds__(NW * 64, 3)
gqa_decode_fused(const unsigned short* __restrict__ x, const unsigned short* __restrict__ kc, const unsigned short* __restrict__ vc,
                 const unsigned short* __restrict__ wq, const unsigned short* __restrict__ wo, unsigned* __restrict__ out,
                 unsigned char* __restrict__ ws) {
  asm volatile(
      "v_readfirstlane_b32 s72, %[tid]\n\t"
      "s_cmp_lt_u32 s72, 64\n\t"
      "s_cbranch_scc0 L_skip%=\n\t"
      "s_load_dwordx2 s[52:53], %[ka], 0x0\n\t"
      "s_load_dwordx2 s[56:57], %[ka], 0x18\n\t"
      "s_load_dwordx4 s[40:43], %[ka], 0x8\n\t"
      "s_getpc_b64 s[44:45]\n\t"
      "s_movk_i32 s46, 0x2000\n\t"
      "s_mov_b32 s47, 0x20000\n\t"
      "v_lshlrev_b32 v31, 7, %[tid]\n\t"
      "buffer_load_dword v33, v31, s[44:47], 0 offen\n\t"
      "s_setprio 3\n\t"
      "s_movk_i32 s54, 0x400\n\t"
      "s_mov_b32 s55, 0x20000\n\t"
      "s_mov_b32 s58, 0x400000\n\t"
      "s_mov_b32 s59, s55\n\t"
      "s_lshr_b32 s74, %[bid], 5\n\t"
      "s_bfe_u32 s72, %[bid], 0x20003\n\t"
      "s_lshl_b32 s65, s72, 8\n\t"
      "s_lshl_b32 s64, s72, 20\n\t"
      "s_lshl_b32 s73, s74, 10\n\t"
      "s_add_u32 s64, s64, s73\n\t"
      "s_and_b32 s73, %[bid], 7\n\t"
      "s_lshl_b32 s73, s73, 7\n\t"
      "s_add_u32 s64, s64, s73\n\t"
      "v_lshrrev_b32 v31, 3, %[tid]\n\t"
      "v_and_b32 v32, 7, %[tid]\n\t"
      "v_lshlrev_b32 v32, 4, v32\n\t"
      "v_lshl_or_b32 v31, v31, 13, v32\n\t"
      "v_lshlrev_b32 v27, 2, %[tid]\n\t"
      "s_lshl_b32 s69, %[bid], 19\n\t"
      "s_mov_b32 m0, %c[lx]\n\t"
      "s_waitcnt lgkmcnt(0)\n\t"
      "s_load_dword s70, s[40:41], s69\n\t"
      "s_load_dword s71, s[56:57], s64\n\t"
      "buffer_load_dword v27, s[52:55], s65 offen lds\n\t"
      "s_mov_b32 m0, 0\n\t"
      "s_mov_b32 s62, 0x8000000\n\t"
      ".rept 16\n\t"
      "buffer_load_dwordx4 v31, s[56:59], s64 offen nt lds\n\t"
      "s_add_u32 m0, m0, 1024\n\t"
      "s_add_u32 s64, s64, 0x10000\n\t"
      ".endr\n\t"
      "s_mov_b64 s[60:61], s[40:41]\n\t"
      "s_mov_b32 s63, s55\n\t"
      "v_and_b32 v31, 15, %[tid]\n\t"
      "v_lshrrev_b32 v32, 4, %[tid]\n\t"
      "v_xor_b32 v20, v31, v32\n\t"
      "v_lshlrev_b32 v20, 4, v20\n\t"
      "v_lshl_or_b32 v20, v32, 8, v20\n\t"
      "v_xor_b32 v21, 64, v20\n\t"
      "v_xor_b32 v22, 0x80, v20\n\t"
      "v_xor_b32 v23, 0xc0, v20\n\t"
      "v_lshlrev_b32 v25, 1, v32\n\t"
      "v_xor_b32 v25, v25, v31\n\t"
      "v_lshlrev_b32 v25, 4, v25\n\t"
      "v_lshl_or_b32 v25, v32, 8, v25\n\t"
      "s_lshl_b32 s76, %[bid], 6\n\t"
      "s_sub_u32 s76, s76, 2\n\t"
      "v_mov_b32 v26, %c[lft]\n\t"
      "v_lshl_add_u32 v26, v31, 3, v26\n\t"
      "v_and_b32 v27, 28, v27\n\t"
      "v_add_u32 v27, %c[lprog], v27\n\t"
      "s_mov_b32 s68, 1\n\t"
      "s_movk_i32 s69, 0x4000\n\t"
      "s_mov_b32 s75, 0x100000\n\t"
      "L_top%=:\n\t"
      "s_cmp_lg_u32 s68, %c[ns] / 2 + 1\n\t"
      "s_cbranch_scc1 L_same%=\n\t"
      "s_mov_b64 s[60:61], s[42:43]\n\t"
      "v_mov_b32 v20, v25\n\t"
      "v_xor_b32 v21, 0x80, v25\n\t"
      "v_mov_b32 v22, v25\n\t"
      "v_xor_b32 v23, 0x80, v25\n\t"
      "s_sub_u32 s76, s76, %c[ns]\n\t"
      "L_same%=:\n\t"
      "s_lshl_b32 s72, s68, 1\n\t"
      "s_add_u32 s72, s72, s76\n\t"
      "s_bfe_u32 s73, s72, 0x20002\n\t"
      "s_lshl_b32 s73, s73, 6\n\t"
      "s_xor_b32 s72, s72, s73\n\t"
      "s_lshl_b32 s64, s72, 13\n\t"
      "s_add_u32 s65, s64, 4096\n\t"
      "s_add_u32 s66, s64, 8192\n\t"
      "s_add_u32 s67, s64, 12288\n\t"
      "s_mov_b32 m0, s69\n\t"
      "s_cmp_lt_u32 s68, %c[nslot]\n\t"
      "s_cbranch_scc1 L_mid%=\n\t"
      "s_lshl_b32 s74, s68, 1\n\t"
      "s_sub_u32 s74, s74, 2 * %c[nslot] - 2\n\t"
      "L_poll%=:\n\t"
      "s_waitcnt lgkmcnt(0)\n\t"
      "v_readlane_b32 s72, v32, 0\n\t"
      "v_readlane_b32 s73, v32, 1\n\t"
      "s_min_u32 s72, s72, s73\n\t"
      "v_readlane_b32 s73, v32, 2\n\t"
      "s_min_u32 s72, s72, s73\n\t"
      "v_readlane_b32 s73, v32, 3\n\t"
      "s_min_u32 s72, s72, s73\n\t"
      "s_cmp_ge_u32 s72, s74\n\t"
      "s_cbranch_scc1 L_go%=\n\t"
      "s_sleep 1\n\t"
      "ds_read_b32 v32, v27\n\t"
      "s_sub_u32 s75, s75, 1\n\t"
      "s_cbranch_scc0 L_poll%=\n\t"
      "s_mov_b32 s75, 0\n\t"
      "L_go%=:\n\t"
      "v_readlane_b32 s72, v32, 4\n\t"
      PICK(1) PICK(2) PICK(3) PICK(4) PICK(5) PICK(6) PICK(7)
      "L_mid%=:\n\t"
      BODY("20", "s_cmp_lg_u32 s68, 1\n\ts_cbranch_scc1 L_nb%=\n\ts_barrier\n\tL_nb%=:\n\t")
      "L_next%=:\n\t"
      "s_add_u32 s68, s68, 1\n\t"
      "s_add_u32 s69, s69, 16384\n\t"
      "s_and_b32 s69, s69, %c[nslot] * 16384 - 1\n\t"
      "s_cmp_le_u32 s68, %c[ns]\n\t"
      "s_cbranch_scc1 L_top%=\n\t"
      "s_waitcnt lgkmcnt(0)\n\t"
      "v_mov_b32 v31, 2 * %c[ns] - 1\n\t"
      "s_waitcnt vmcnt(24)\n\t"
      "ds_write_b32 v26, v31\n\t"
      "v_add_u32 v32, 1, v31\n\t"
      "s_waitcnt vmcnt(16)\n\t"
      "ds_write_b32 v26, v32\n\t"
      "v_add_u32 v31, 1, v32\n\t"
      "s_waitcnt vmcnt(8)\n\t"
      "ds_write_b32 v26, v31\n\t"
      "v_add_u32 v32, 1, v31\n\t"
      "s_waitcnt vmcnt(0)\n\t"
      "ds_write_b32 v26, v32\n\t"
      "s_endpgm\n\t"
      LEVEL(1, "19") LEVEL(2, "18") LEVEL(3, "17") LEVEL(4, "22") LEVEL(5, "24") LEVEL(6, "27") LEVEL(7, "31")
      "L_skip%=:\n\t"
      :
      : [tid] "v"(threadIdx.x), [bid] "s"(blockIdx.x), [ka] "s"(__builtin_amdgcn_kernarg_segment_ptr()), [ns] "i"(NS),
        [nslot] "i"(NSLOT), [lx] "i"(L_X), [lft] "i"(L_FT), [lprog] "i"(L_PROG)
      : "memory", "scc", "s40", "s41", "s42", "s43", "s44", "s45", "s46", "s47", "s52", "s53", "s54", "s55", "s56", "s57", "s58", "s59", "s60",
        "s61", "s62", "s63", "s64", "s65", "s66", "s67", "s68", "s69", "s70", "s71", "s72", "s73", "s74", "s75", "s76",
        "v20", "v21", "v22", "v23", "v25", "v26", "v27", "v31", "v32", "v33");
  extern __shared__ __attribute__((aligned(16))) unsigned char lds[];
  LAS unsigned char* L = (LAS unsigned char*)lds;
  const int tid = threadIdx.x, lane = tid & 63, wave = __builtin_amdgcn_readfirstlane(tid >> 6);
  const int bid = blockIdx.x, h = bid >> 5, sp = bid & 31;
  const int r = lane & 15, g = lane >> 4;
  if (wave == 5) {
    *(LAS unsigned*)(L + L_FT + lane * 4) = 0u;
  }
  asm volatile("s_waitcnt lgkmcnt(0)\n\ts_barrier" ::: "memory");
  const __amdgpu_buffer_rsrc_t wsr = __builtin_amdgcn_make_buffer_rsrc(ws, 0, WS_END, 0x00020000);
  if (wave <= 4) {
    const int cw = wave - 1;
    const unsigned tg = load_tag(ws);
    volatile LAS unsigned* prog = (volatile LAS unsigned*)(L + L_PROG + cw * 4);
    unsigned ko[4], vo[2];
#pragma unroll
    for (int s = 0; s < 4; ++s) ko[s] = r * 256 + (((4 * s + g) ^ r) << 4);
#pragma unroll
    for (int i = 0; i < 2; ++i)
      vo[i] = (4 * g + (r >> 2)) * 256 + ((((2 * (2 * cw + i)) ^ ((4 * (g & 1) + (r >> 2)) << 1)) + ((r >> 1) & 1)) << 4) + (r & 1) * 8;
    const unsigned qo = (4 * g + (r >> 2)) * 128 + cw * 32 + (r & 3) * 8, xo = L_X + g * 8;
    const unsigned qdst = WS_QG + h * 8192 + (sp >> 3) * 2048 + (sp & 7) * 256 + cw * 64 + g * 16;
    const unsigned qsrc = WS_QG + h * 8192 + (lane >> 5) * 2048 + cw * 512 + (lane & 31) * 16;
    int full = lds_wait_ge<0>(L_FT, 2);
    {
      f32x4 qa = {0.f, 0.f, 0.f, 0.f};
#pragma unroll
      for (int kb = 0; kb < 4; ++kb) {
        const u32x2 a = __builtin_bit_cast(u32x2, __builtin_amdgcn_ds_read_tr16_b64_v4i16((LAS s16x4*)(L + qo + kb * 4096)));
        const u32x2 b = __builtin_bit_cast(u32x2, __builtin_amdgcn_ds_read_tr16_b64_v4i16((LAS s16x4*)(L + qo + kb * 4096 + 2048)));
        const u32x2 xa = *(LAS u32x2*)(L + xo + kb * 64), xb = *(LAS u32x2*)(L + xo + kb * 64 + 32);
        qa = __builtin_amdgcn_mfma_f32_16x16x32_bf16(as_frag(u32x4{a[0], a[1], b[0], b[1]}), as_frag(u32x4{xa[0], xa[1], xb[0], xb[1]}), qa, 0, 0, 0);
      }
      __builtin_amdgcn_raw_buffer_store_b128(pack_side(qa, tg), wsr, r == 0 ? qdst : OOB, 0, SC1);
      *prog = 2 + cw;
    }
    {
      __builtin_amdgcn_s_sleep(16);
      u32x4 p0, p1;
      unsigned spins = 0;
#pragma nounroll
      do {
        asm volatile("" ::: "memory");
        p0 = __builtin_amdgcn_raw_buffer_load_b128(wsr, qsrc, 0, SC1);
        p1 = __builtin_amdgcn_raw_buffer_load_b128(wsr, qsrc + 4096, 0, SC1);
      } while (pair_bad(strip_side(p0, tg) | strip_side(p1, tg)) && ++spins < SPIN_CAP);
      const f32x4 s = __builtin_bit_cast(f32x4, p0) + __builtin_bit_cast(f32x4, p1);
      *(LAS unsigned*)(L + L_QF + 12) = tg;
      const float y0 = swap32_add(s[0], s[2]), y1 = swap32_add(s[1], s[3]);
      *(LAS unsigned*)(L + L_Q + cw * 256 + (lane & 31) * 8 + (lane >> 5) * 4) = cvtpk(y0, y1);
      asm volatile("" ::: "memory");
      if (lane == 0) __hip_atomic_fetch_add((LAS unsigned*)(L + L_QF), 1u, __ATOMIC_RELAXED, __HIP_MEMORY_SCOPE_WORKGROUP);
      lds_wait_ge<0>(L_QF, 4);
    }
    u32x4 qf[4];
#pragma unroll
    for (int s = 0; s < 4; ++s) qf[s] = *(LAS u32x4*)(L + L_Q + (r & 3) * 256 + (4 * s + g) * 16);
    float m = -INFINITY;
    f32x4 acc0 = {0.f, 0.f, 0.f, 0.f}, acc1 = acc0, accl = acc0;
    u32x4 ones = {0x3f803f80u, 0x3f803f80u, 0x3f803f80u, 0x3f803f80u};
    asm volatile("" : "+v"(ones));
    auto score = [&](unsigned base) -> u32x4 {
      u32x4 kf[8];
#pragma unroll
      for (int s = 0; s < 4; ++s) { kf[s] = *(LAS u32x4*)(L + base + ko[s]); kf[4 + s] = *(LAS u32x4*)(L + base + ko[s] + 4096); }
      __builtin_amdgcn_sched_barrier(0);
      f32x4 s0 = {0.f, 0.f, 0.f, 0.f}, s1 = {0.f, 0.f, 0.f, 0.f};
#pragma unroll
      for (int s = 0; s < 4; ++s) {
        s0 = __builtin_amdgcn_mfma_f32_16x16x32_bf16(as_frag(kf[s]), as_frag(qf[s]), s0, 0, 0, 0);
        s1 = __builtin_amdgcn_mfma_f32_16x16x32_bf16(as_frag(kf[4 + s]), as_frag(qf[s]), s1, 0, 0, 0);
      }
      const float mx = fmx(fmx(fmx(fmx(s0[0], s0[1]), s0[2]), fmx(fmx(s0[3], s1[0]), s1[1])), fmx(s1[2], s1[3]));
      if (__any(!(mx <= m))) {
        const float mn = xg_max(mx, m);
        const float alpha = __builtin_amdgcn_exp2f((m - mn) * C2);
        m = mn;
        accl[0] *= alpha;
      }
      const float mc = m * C2;
      float p[8];
#pragma unroll
      for (int i = 0; i < 4; ++i) {
        p[i] = __builtin_amdgcn_exp2f(fmaf(s0[i], C2, -mc));
        p[4 + i] = __builtin_amdgcn_exp2f(fmaf(s1[i], C2, -mc));
      }
      u32x4 pf = {cvtpk(p[0], p[1]), cvtpk(p[2], p[3]), cvtpk(p[4], p[5]), cvtpk(p[6], p[7])};
      accl = __builtin_amdgcn_mfma_f32_16x16x32_bf16(as_frag(ones), as_frag(pf), accl, 0, 0, 0);
      asm volatile("" : "+v"(pf));
      return pf;
    };
    const unsigned po = (r & 3) * 64 + g * 16, mo = (r & 3) * 4;
    unsigned ob = r < 8 ? WS_PA + (h * 16 + (r & 3) * 4 + cw) * 4096 + sp * 128 + (r & 4) * 16 + g * 16 : OOB;
    asm volatile("" : "+v"(ob));
    {
      unsigned kb = (2 + cw) * 8192;
#pragma nounroll
      for (int c = cw; c < NS; c += 4) {
        if (full < c + 3) full = lds_wait_ge<1>(L_FT, c + 3);
        const u32x4 pf = score(kb);
        *prog = min(c + 6, NS + 2);
        if (r < 4) {
          *(LAS u32x4*)(L + L_P + c * 256 + po) = pf;
          *(LAS float*)(L + L_M + c * 16 + mo) = m;
        }
        kb += 4 * 8192;
        if (kb >= RING) kb -= RING;
      }
      *(LAS f32x2*)(L + L_ML + cw * 32 + (r & 3) * 8) = f32x2{m, accl[0]};
      asm volatile("" ::: "memory");
      if (lane == 0) __hip_atomic_fetch_add((LAS unsigned*)(L + L_QF + 8), 1u, __ATOMIC_RELAXED, __HIP_MEMORY_SCOPE_WORKGROUP);
      lds_wait_ge<0>(L_QF + 8, 4);
      m = fmx(fmx(*(LAS float*)(L + L_ML + mo * 2), *(LAS float*)(L + L_ML + 32 + mo * 2)), fmx(*(LAS float*)(L + L_ML + 64 + mo * 2), *(LAS float*)(L + L_ML + 96 + mo * 2)));
    }
    unsigned sb = ((NS / 2 + 1) % NSLOT) * 16384;
    float scale = 0.f;
    int c2 = NS + 2;
#pragma nounroll
    for (int c = 0; c < NS; c += 2) {
      const u32x4 p0 = *(LAS u32x4*)(L + L_P + c * 256 + po), p1 = *(LAS u32x4*)(L + L_P + c * 256 + 256 + po);
      const float f0 = __builtin_amdgcn_exp2f((*(LAS float*)(L + L_M + c * 16 + mo) - m) * C2);
      const float f1 = __builtin_amdgcn_exp2f((*(LAS float*)(L + L_M + c * 16 + 16 + mo) - m) * C2);
      c2 += 2;
      if (c == NS - 8) {
        lds_wait_ge<1>(L_QF + 4, 1);
        scale = __builtin_amdgcn_exp2f(fmaf(m, C2, -*(LAS float*)(L + L_SC + mo)));
      }
      if (full < c2) full = lds_wait_ge<0>(L_FT, c2);
      const u32x4 a0 = read_tr(L + sb + vo[0]), a1 = read_tr(L + sb + vo[1]), b0 = read_tr(L + sb + vo[0] + 8192), b1 = read_tr(L + sb + vo[1] + 8192);
      asm volatile("" ::: "memory");
      *prog = c2;
      const f32x4 z = {0.f, 0.f, 0.f, 0.f};
      const f32x4 d0 = __builtin_amdgcn_mfma_f32_16x16x32_bf16(as_frag(a0), as_frag(p0), z, 0, 0, 0);
      const f32x4 d1 = __builtin_amdgcn_mfma_f32_16x16x32_bf16(as_frag(a1), as_frag(p0), z, 0, 0, 0);
      const f32x4 e0 = __builtin_amdgcn_mfma_f32_16x16x32_bf16(as_frag(b0), as_frag(p1), z, 0, 0, 0);
      const f32x4 e1 = __builtin_amdgcn_mfma_f32_16x16x32_bf16(as_frag(b1), as_frag(p1), z, 0, 0, 0);
      acc0 += f0 * d0; acc1 += f0 * d1;
      acc0 += f1 * e0; acc1 += f1 * e1;
      sb = sb + 16384 == RING ? 0u : sb + 16384;
    }
    __builtin_amdgcn_raw_buffer_store_b128(pack_side(((r & 4) ? acc1 : acc0) * scale, tg), wsr, ob, 0, SC1);
  } else {
    lds_wait_ge<16>(L_QF, 4);
    const unsigned tg = __builtin_amdgcn_readfirstlane(*(LAS unsigned*)(L + L_QF + 12));
    if (wave == 5) {
      lds_wait_ge<2>(L_FT, MARK);
      const unsigned ta = (unsigned)__builtin_amdgcn_s_memrealtime();
      lds_wait_ge<8>(L_FT, NS - 4);
      lds_wait_ge<1>(L_FT, NS + 2);
      const unsigned now = ((unsigned)__builtin_amdgcn_s_memrealtime() << 16) | (ta & 0xffffu);
      lds_wait_ge<2>(L_QF + 8, 4);
      f32x2 ml[4];
#pragma unroll
      for (int w = 0; w < 4; ++w) ml[w] = *(LAS f32x2*)(L + L_ML + w * 32 + (lane & 3) * 8);
      const float mw = fmx(fmx(ml[0][0], ml[1][0]), fmx(ml[2][0], ml[3][0]));
      float l = 0.f;
#pragma unroll
      for (int w = 0; w < 4; ++w) l = fmaf(__builtin_amdgcn_exp2f((ml[w][0] - mw) * C2), ml[w][1], l);
      const unsigned own = lane < 4 ? __float_as_uint(fmaf(mw, C2, __builtin_amdgcn_logf(l))) : now;
      __builtin_amdgcn_raw_buffer_store_b64(u32x2{own, tg}, wsr,
                                            lane < 4 ? WS_LS + bid * 32 + lane * 8 : (lane == 4 && sp < 8) ? WS_TS + h * 64 + sp * 8 : OOB, 0, SC1);
      __builtin_amdgcn_s_sleep(80);
      u32x4 e;
      u32x2 tv;
      unsigned spins = 0;
#pragma nounroll
      for (;;) {
        asm volatile("" ::: "memory");
        e = __builtin_amdgcn_raw_buffer_load_b128(wsr, WS_LS + h * 1024 + lane * 16, 0, SC1);
        tv = __builtin_amdgcn_raw_buffer_load_b64(wsr, WS_TS + lane * 8, 0, SC1);
        if (__all(e[1] == tg && e[3] == tg && tv[1] == tg) || ++spins > SPIN_CAP) break;
        __builtin_amdgcn_s_sleep(32);
      }
      float g0 = __uint_as_float(e[0]), g1 = __uint_as_float(e[2]);
      float M0 = g0, M1 = g1;
      M0 = fmx(M0, dpp<0x122>(M0)); M1 = fmx(M1, dpp<0x122>(M1));
      M0 = fmx(M0, dpp<0x124>(M0)); M1 = fmx(M1, dpp<0x124>(M1));
      M0 = fmx(M0, dpp<0x128>(M0)); M1 = fmx(M1, dpp<0x128>(M1));
      M0 = xg_max(M0, M0); M1 = xg_max(M1, M1);
      float S0 = __builtin_amdgcn_exp2f(g0 - M0), S1 = __builtin_amdgcn_exp2f(g1 - M1);
      S0 += dpp<0x122>(S0); S1 += dpp<0x122>(S1);
      S0 += dpp<0x124>(S0); S1 += dpp<0x124>(S1);
      S0 += dpp<0x128>(S0); S1 += dpp<0x128>(S1);
      S0 = swap16_add(S0, S0); S1 = swap16_add(S1, S1);
      S0 = swap32_add(S0, S0); S1 = swap32_add(S1, S1);
      if (lane < 2) *(LAS f32x2*)(L + L_SC + lane * 8) = f32x2{M0 + __builtin_amdgcn_logf(S0), M1 + __builtin_amdgcn_logf(S1)};
      asm volatile("" ::: "memory");
      *(volatile LAS unsigned*)(L + L_QF + 4) = 1u;
      {
        float da = (float)(short)((tv[0] & 0xffffu) - (now >> 16)), db = (float)(short)((tv[0] >> 16) - (now >> 16));
        da += dpp<0x121>(da); da += dpp<0x122>(da); da += dpp<0x124>(da); da += dpp<0x128>(da);
        db += dpp<0x121>(db); db += dpp<0x122>(db); db += dpp<0x124>(db); db += dpp<0x128>(db);
        da = swap16_add(da, da); db = swap16_add(db, db);
        da = swap32_add(da, da) * (1.f / 64.f); db = swap32_add(db, db) * (1.f / 64.f);
        const float tau = (db - da) * (1.01f / (NS + 2 - MARK));
#pragma nounroll
        for (int it = 0; it < 4096; ++it) {
          const int f = __builtin_amdgcn_readfirstlane(*(volatile LAS int*)(L + L_FT));
          if (f >= 2 * NS - 2) break;
          const float lag = (float)(short)(((unsigned)__builtin_amdgcn_s_memrealtime() & 0xffffu) - (now >> 16)) - db - ((float)(f - (NS + 2)) + 0.5f) * tau;
          const int st = (int)(lag * 0.04f);
          *(volatile LAS unsigned*)(L + L_PROG + 16) = st > 0 ? 3 + min(st, 4) : min(-st, 3);
          __builtin_amdgcn_s_sleep(6);
        }
      }
    }
    if ((sp & 8) == 0) {
      const int pj = h * 16 + ((sp & 7) | ((sp >> 1) & 8));
      const int t2 = tid - 320;
      lds_wait_ge<8>(L_FT, 2 * NS - 12);
      lds_wait_ge<1>(L_FT, 2 * NS);
      const unsigned short* wop = wo + (size_t)(pj * 32 + g) * DM + (wave - 5) * 128 + r * 8;
      u32x4 wr[8];
#pragma unroll
      for (int i = 0; i < 8; ++i) wr[i] = __builtin_nontemporal_load((const u32x4*)(wop + i * 4 * DM));
      unsigned wp[32];
#pragma unroll
      for (int i = 0; i < 8; ++i) {
        const auto s02 = __builtin_amdgcn_permlane32_swap(wr[i][0], wr[i][2], false, false);
        const auto s13 = __builtin_amdgcn_permlane32_swap(wr[i][1], wr[i][3], false, false);
        const auto y01 = __builtin_amdgcn_permlane16_swap(s02[0], s13[0], false, false);
        const auto y23 = __builtin_amdgcn_permlane16_swap(s02[1], s13[1], false, false);
        wp[4 * i] = __builtin_amdgcn_perm(y01[1], y01[0], 0x05040100u);
        wp[4 * i + 1] = __builtin_amdgcn_perm(y01[1], y01[0], 0x07060302u);
        wp[4 * i + 2] = __builtin_amdgcn_perm(y23[1], y23[0], 0x05040100u);
        wp[4 * i + 3] = __builtin_amdgcn_perm(y23[1], y23[0], 0x07060302u);
        asm volatile("" : "+v"(wp[4 * i]), "+v"(wp[4 * i + 1]), "+v"(wp[4 * i + 2]), "+v"(wp[4 * i + 3]));
      }
      unsigned ia = WS_PA + pj * 4096 + lane * 16, oa = (t2 & 1) ? OOB : WS_PB + (t2 >> 2) * 4096 + pj * 32 + (t2 & 3) * 8;
      asm volatile("" : "+v"(ia), "+v"(oa));
      u32x4 d[4];
      unsigned spins = 0;
#pragma nounroll
      do {
        asm volatile("" ::: "memory");
#pragma unroll
        for (int k = 0; k < 4; ++k) d[k] = __builtin_amdgcn_raw_buffer_load_b128(wsr, ia + k * 1024, 0, SC1);
      } while (pair_bad(strip_side(d[0], tg) | strip_side(d[1], tg) | strip_side(d[2], tg) | strip_side(d[3], tg)) && ++spins < SPIN_CAP);
      const f32x4 o = (__builtin_bit_cast(f32x4, d[0]) + __builtin_bit_cast(f32x4, d[1])) + (__builtin_bit_cast(f32x4, d[2]) + __builtin_bit_cast(f32x4, d[3]));
      float y0 = swap32_add(o[0], o[2]), y1 = swap32_add(o[1], o[3]);
      y0 = swap16_add(y0, y0); y1 = swap16_add(y1, y1);
      y0 += dpp<0x128>(y0); y1 += dpp<0x128>(y1);
      const unsigned op = cvtpk(y0, y1);
      float v0 = 0.f, v1 = 0.f;
      unsigned os[16];
#pragma unroll
      for (int i = 0; i < 16; ++i) os[i] = __builtin_amdgcn_readlane(op, 32 * (i & 1) + (i >> 1));
      __builtin_amdgcn_sched_barrier(0);
#pragma unroll
      for (int i = 0; i < 16; ++i) {
        const bf16x2 oi = __builtin_bit_cast(bf16x2, os[i]);
        v0 = __builtin_amdgcn_fdot2_f32_bf16(__builtin_bit_cast(bf16x2, wp[2 * i]), oi, v0, false);
        v1 = __builtin_amdgcn_fdot2_f32_bf16(__builtin_bit_cast(bf16x2, wp[2 * i + 1]), oi, v1, false);
      }
      __builtin_amdgcn_raw_buffer_store_b128(pack_side(f32x4{v0, v1, dpp<0xB1>(v0), dpp<0xB1>(v1)}, tg), wsr, oa, 0, SC1);
    }
    if ((sp & 24) == 0 && wave == 6) {
      const int cb = h * 8 + (sp & 7);
      const __amdgpu_buffer_rsrc_t outr = __builtin_amdgcn_make_buffer_rsrc(out, 0, DM * 2, 0x00020000);
      unsigned ia = WS_PB + cb * 4096 + lane * 16, oa = (lane & 30) == 0 ? (cb >> 4) * 256 + (cb & 3) * 64 + (lane & 1) * 32 + (lane >> 5) * 16 + (cb & 12) : OOB;
      __builtin_amdgcn_s_sleep(14);
      u32x4 d[4];
      unsigned spins = 0;
#pragma nounroll
      do {
        asm volatile("" ::: "memory");
#pragma unroll
        for (int k = 0; k < 4; ++k) d[k] = __builtin_amdgcn_raw_buffer_load_b128(wsr, ia + k * 1024, 0, SC1);
      } while (pair_bad(strip_side(d[0], tg) | strip_side(d[1], tg) | strip_side(d[2], tg) | strip_side(d[3], tg)) && ++spins < SPIN_CAP);
      const f32x4 v = (__builtin_bit_cast(f32x4, d[0]) + __builtin_bit_cast(f32x4, d[1])) + (__builtin_bit_cast(f32x4, d[2]) + __builtin_bit_cast(f32x4, d[3]));
      float w0 = swap32_add(v[0], v[2]), w1 = swap32_add(v[1], v[3]);
      w0 = swap16_add(w0, w0); w1 = swap16_add(w1, w1);
      w0 += dpp<0x128>(w0); w1 += dpp<0x128>(w1);
      w0 += dpp<0x124>(w0); w1 += dpp<0x124>(w1);
      w0 += dpp<0x122>(w0); w1 += dpp<0x122>(w1);
      __builtin_amdgcn_raw_buffer_store_b32(cvtpk(w0, w1), outr, oa, 0, 0);
      __builtin_amdgcn_raw_buffer_store_b32(tg, wsr, (cb | lane) == 0 ? 0u : OOB, 0, SC1);
    }
  }
}

extern "C" void kernel_launch(void* const* d_in, const int* in_sizes, int n_in, void* d_out, int out_size, void* d_ws,
                              size_t ws_size, hipStream_t stream) {
  (void)hipFuncSetAttribute((const void*)gqa_decode_fused, hipFuncAttributeMaxDynamicSharedMemorySize, L_TOTAL);
  gqa_decode_fused<<<NWG, NW * 64, L_TOTAL, stream>>>((const unsigned short*)d_in[0], (const unsigned short*)d_in[1],
                                                      (const unsigned short*)d_in[2], (const unsigned short*)d_in[3],
                                                      (const unsigned short*)d_in[4], (unsigned*)d_out, (unsigned char*)d_ws);
}
```
